```python
import torch

def _softmax(x, axis=-1):
    return torch.softmax(x, dim=axis)

def _gelu(x):
    return torch.nn.functional.gelu(x, approximate='tanh')

def _silu(x):
    return torch.nn.functional.silu(x)

def _sigmoid(x):
    return torch.sigmoid(x)

def _layernorm(x, axis=-1, eps=1e-05):
    m = x.mean(dim=axis, keepdim=True)
    v = x.var(dim=axis, keepdim=True, unbiased=False)
    return (x - m) / torch.sqrt(v + eps)

def _scatter_add(tgt, idx, src, axis=0):
    return tgt.clone().index_add_(axis, idx, src)

def _segment_sum(src, seg, n):
    out = torch.zeros((n,) + src.shape[1:], dtype=src.dtype)
    return out.index_add_(0, seg, src)

def _pad(x, pw):
    flat = tuple((v for pair in reversed(pw) for v in pair))
    return torch.nn.functional.pad(x, flat)
kernel_name = 'gated_mix__memory_aug_recurrent_mem_tr__moe_arch_mod__prompt'

def setup_inputs():

    _w = lambda a: (lambda t: t.to(torch.bfloat16) if t.is_floating_point() else t)(torch.as_tensor(a))

    def _A_setup():
        B, S, Dm = (1, 2048, 512)
        H, Dh = (32, 128)
        M, seg = (8, 32)
        seg = min(seg, S)
        S = S // seg * seg
        return {'x': _w((lambda *s: torch.randn(*s, dtype=torch.bfloat16) * 0.05)(B, S, Dm)), 'mem0': _w((lambda *s: torch.randn(*s, dtype=torch.bfloat16) * 0.8)(B, M, Dm)), 'Wq': _w((lambda *s: torch.randn(*s, dtype=torch.bfloat16) * 3.2)(Dm, H, Dh) * Dm ** (-0.5)), 'Wk': _w((lambda *s: torch.randn(*s, dtype=torch.bfloat16) * 3.2)(Dm, H, Dh) * Dm ** (-0.5)), 'Wv': _w((lambda *s: torch.randn(*s, dtype=torch.bfloat16) * 0.8)(Dm, H, Dh) * Dm ** (-0.5)), 'Wo': _w((lambda *s: torch.randn(*s, dtype=torch.bfloat16) * 0.8)(H, Dh, Dm) * (H * Dh) ** (-0.5)), 'seg_len': seg}
    _A_in = _A_setup()

    def _B_setup():
        B, S, D, F, C = (1, 2048, 512, 1792, 8)
        return {'x': _w((lambda *s: torch.randn(*s, dtype=torch.bfloat16) * 0.05)(B, S, D)), 'w_r': _w((lambda *s: torch.randn(*s, dtype=torch.bfloat16) * 1.6)(D)), 'w_gate': _w((lambda *s: torch.randn(*s, dtype=torch.bfloat16) * 0.2)(D, F)), 'w_up': _w((lambda *s: torch.randn(*s, dtype=torch.bfloat16) * 0.2)(D, F)), 'w_down': _w((lambda *s: torch.randn(*s, dtype=torch.bfloat16) * 0.2)(F, D))}
    _B_in = _B_setup()
    return {'A_x': _A_in['x'], 'A_mem0': _A_in['mem0'], 'A_Wq': _A_in['Wq'], 'A_Wk': _A_in['Wk'], 'A_Wv': _A_in['Wv'], 'A_Wo': _A_in['Wo'], 'A_seg_len': _A_in['seg_len'], 'B_x': _B_in['x'], 'B_w_r': _B_in['w_r'], 'B_w_gate': _B_in['w_gate'], 'B_w_up': _B_in['w_up'], 'B_w_down': _B_in['w_down'], 'gate_w': _w((lambda *s: torch.randn(*s, dtype=torch.bfloat16) * 0.05)(512) * 16.0)}

def reference(A_x, A_mem0, A_Wq, A_Wk, A_Wv, A_Wo, A_seg_len, B_x, B_w_r, B_w_gate, B_w_up, B_w_down, gate_w):

    def _A_ref(x, mem0, Wq, Wk, Wv, Wo, seg_len):
        B, S, Dm = x.shape
        H, Dh = (Wq.shape[1], Wq.shape[2])
        M = mem0.shape[1]
        scale = Dh ** (-0.5)
        n_seg = S // seg_len
        L = M + seg_len
        idx = torch.arange(L)
        mask = (idx[:, None] < M) | (idx[:, None] >= idx[None, :])
        mem = mem0
        outs = []
        for s in range(n_seg):
            xs = x[:, s * seg_len:(s + 1) * seg_len, :].clone()
            h = torch.concatenate([mem, xs], axis=1)
            q = torch.einsum('btd,dhe->bhte', h, Wq)
            k = torch.einsum('btd,dhe->bhte', h, Wk)
            v = torch.einsum('btd,dhe->bhte', h, Wv)
            scores = torch.einsum('bhqe,bhke->bhqk', q, k) * scale
            scores = torch.where(mask[None, None], scores, -1000000000.0)
            p = _softmax(scores, axis=-1)
            o = torch.einsum('bhqk,bhke->bhqe', p, v)
            o = torch.einsum('bhte,hed->btd', o, Wo)
            mem = o[:, :M, :].clone()
            outs.append(o[:, M:, :])
        return torch.concatenate(outs, axis=1)

    def _B_ref(x, w_r, w_gate, w_up, w_down):
        B, S, D = x.shape
        C = 8
        r = x @ w_r
        idx = torch.argsort(-r, axis=-1)[:, :C]
        idx_e = (lambda x, n, axis: torch.repeat_interleave(x, n, dim=axis))(idx.reshape(B, C, 1), D, axis=2)
        x_sel = (lambda x, i, axis: torch.take_along_dim(x, i, dim=axis))(x, idx_e, axis=1)
        g = _silu(x_sel @ w_gate)
        u = x_sel @ w_up
        y = g * u @ w_down
        r_sel = _sigmoid((lambda x, i, axis: torch.take_along_dim(x, i, dim=axis))(r, idx, axis=1))
        y = y * r_sel.reshape(B, C, 1)
        b_off = (torch.arange(B) * S).reshape(B, 1)
        flat_idx = (idx + b_off).reshape(B * C)
        zero = (x * 0.0).reshape(B * S, D)
        upd = _scatter_add(zero, flat_idx, y.reshape(B * C, D), axis=0)
        return x + upd.reshape(B, S, D)
    x = A_x
    g = _sigmoid((x * gate_w).sum(-1, keepdims=True))
    a = _A_ref(x, A_mem0, A_Wq, A_Wk, A_Wv, A_Wo, A_seg_len)
    b = _B_ref(x, B_w_r, B_w_gate, B_w_up, B_w_down)
    return g * a + (1 - g) * b
```

```cpp
#include <hip/hip_runtime.h>
#include <hip/hip_bf16.h>
#include <stdint.h>

#define LAS __attribute__((address_space(3)))
#define GAS __attribute__((address_space(1)))
typedef unsigned int u32;
typedef unsigned short u16;
typedef __bf16 bf16x2 __attribute__((ext_vector_type(2)));
typedef __bf16 bf16x4 __attribute__((ext_vector_type(4)));
typedef __bf16 bf16x8 __attribute__((ext_vector_type(8)));
typedef __bf16 bf16x16 __attribute__((ext_vector_type(16)));
typedef short s16x4 __attribute__((ext_vector_type(4)));
typedef float f32x2 __attribute__((ext_vector_type(2)));
typedef float f32x4 __attribute__((ext_vector_type(4)));
typedef u32 u32x2 __attribute__((ext_vector_type(2)));
typedef u32 u32x4 __attribute__((ext_vector_type(4)));
typedef __amdgpu_buffer_rsrc_t rsrc_t;
typedef LAS unsigned char* ldsp;

constexpr int SEQ = 2048, DM = 512, NH = 32, DH = 128, NSEG = 64, FF = 1792;
constexpr int WP = NH * DH;

constexpr u32 OFF_ABORT = 1024;
constexpr u32 OFF_PSEL = 1152;
constexpr u32 OFF_FKV = 2048;
constexpr u32 OFF_FKM = 6144;
constexpr u32 OFF_FQA = 10240;
constexpr u32 OFF_FY = 18432;
constexpr u32 OFF_RG = 24576;
constexpr u32 OFF_MEMR = 32768;
constexpr u32 OFF_PROBE = 98304;
constexpr u32 OFF_REF = OFF_PROBE + 4096;
constexpr u32 OFF_POST = 114688;
constexpr u32 OFF_CLAIM = 131072;
constexpr u32 CTRL_BYTES = 163840;
constexpr u32 OFF_PART = 2u << 20;
constexpr u32 PART_SLOT = 512u << 10;
constexpr u32 OFF_KV = 4u << 20;
constexpr u32 OFF_KM = 36u << 20;
constexpr u32 OFF_YP = 44u << 20;
constexpr u32 OFF_XP = 48u << 20;
constexpr u32 WS_USED = 112u << 20;
constexpr u32 SENT = 0xFFFFFFFFu;
constexpr u32 SPIN_LIMIT = 1u << 17;

constexpr int LDS_BYTES = 160 * 1024;


struct Params {
  const u16* x; const u16* mem0; const u16* wq; const u16* wk; const u16* wv; const u16* wo;
  const u16* wr; const u16* wg; const u16* wu; const u16* wd; const u16* gw;
  u16* out; unsigned char* ws;
};

__device__ __forceinline__ u32 pk2(float a, float b) {
  return __builtin_bit_cast(u32, __builtin_convertvector((f32x2){a, b}, bf16x2));
}
__device__ __forceinline__ float bflo(u32 u) { return __uint_as_float(u << 16); }
__device__ __forceinline__ float bfhi(u32 u) { return __uint_as_float(u & 0xffff0000u); }
__device__ __forceinline__ float bfr(float a) { return (float)(__bf16)a; }
__device__ __forceinline__ u32 nosent(u32 u) { return u < 0xFFFFFFFEu ? u : 0xFFFFFFFEu; }
__device__ __forceinline__ u32x4 nosent4(u32x4 v) { return __builtin_elementwise_min(v, (u32x4){0xFFFFFFFEu, 0xFFFFFFFEu, 0xFFFFFFFEu, 0xFFFFFFFEu}); }
__device__ __forceinline__ void bar() {
  asm volatile("" ::: "memory");
  __builtin_amdgcn_s_waitcnt(0xC07F);
  __builtin_amdgcn_s_barrier();
  asm volatile("" ::: "memory");
}
__device__ __forceinline__ void rawbar() { asm volatile("s_barrier" ::: "memory"); }
__device__ __forceinline__ void drain() { asm volatile("s_waitcnt vmcnt(0)" ::: "memory"); }
__device__ __forceinline__ u32x4 ld16(rsrc_t r, u32 off) { return __builtin_amdgcn_raw_buffer_load_b128(r, off, 0, 16); }
__device__ __forceinline__ u32x4 ld16s(rsrc_t r, u32 off, u32 soff) { return __builtin_amdgcn_raw_buffer_load_b128(r, off, soff, 16); }
__device__ __forceinline__ void st16s(rsrc_t r, u32 off, u32 soff, u32x4 v) { __builtin_amdgcn_raw_buffer_store_b128(v, r, off, soff, 16); }
__device__ __forceinline__ u32 ld4(rsrc_t r, u32 off) { return __builtin_amdgcn_raw_buffer_load_b32(r, off, 0, 16); }
__device__ __forceinline__ void st16(rsrc_t r, u32 off, u32x4 v) { __builtin_amdgcn_raw_buffer_store_b128(v, r, off, 0, 16); }
__device__ __forceinline__ void st8(rsrc_t r, u32 off, u32x2 v) { __builtin_amdgcn_raw_buffer_store_b64(v, r, off, 0, 16); }
__device__ __forceinline__ void st4(rsrc_t r, u32 off, u32 v) { __builtin_amdgcn_raw_buffer_store_b32(v, r, off, 0, 16); }
__device__ __forceinline__ bool ok4(u32x4 v) { return (v.x != SENT) & (v.y != SENT) & (v.z != SENT) & (v.w != SENT); }
__device__ __forceinline__ bool any_sent(u32x4 v) {
  const u32 a = v.x > v.y ? v.x : v.y, b = v.z > v.w ? v.z : v.w;
  return __builtin_amdgcn_ballot_w64((a > b ? a : b) == SENT) != 0;
}
__device__ __forceinline__ u32x4 umax4(u32x4 a, u32x4 b) { return __builtin_elementwise_max(a, b); }
__device__ __forceinline__ bf16x8 lds8(ldsp p) { return *(LAS bf16x8*)p; }
__device__ __forceinline__ float swadd32(float a, float b) {
  auto r = __builtin_amdgcn_permlane32_swap(__float_as_uint(a), __float_as_uint(b), false, false);
  return __uint_as_float(r[0]) + __uint_as_float(r[1]);
}
__device__ __forceinline__ float swadd16(float a, float b) {
  auto r = __builtin_amdgcn_permlane16_swap(__float_as_uint(a), __float_as_uint(b), false, false);
  return __uint_as_float(r[0]) + __uint_as_float(r[1]);
}
__device__ __forceinline__ float xsum(float a) {
  a = swadd16(a, a);
  return swadd32(a, a);
}
__device__ __forceinline__ float sigmoidf_(float z) { return 1.0f / (1.0f + __expf(-z)); }

struct Spin {
  rsrc_t r; u32 dead;
  __device__ __forceinline__ u32 enc(int v) const { return (u32)v; }
  __device__ __forceinline__ int dec(u32 x) const { return (int)x; }
  __device__ __forceinline__ bool fail(u32& n) {
    if (dead) return true;
    ++n;
    if ((n & 127u) == 0u) {
      if (n > SPIN_LIMIT) { st4(r, OFF_ABORT, enc(0)); dead = 1; }
      else if ((u32)__builtin_amdgcn_readfirstlane(ld4(r, OFF_ABORT)) == enc(0)) dead = 1;
    }
    return dead != 0;
  }
  __device__ __forceinline__ int wait_flag(u32 off, int want, int sleep) {
    u32 n = 0;
    for (;;) {
      asm volatile("" ::: "memory");
      const int v = dec(__builtin_amdgcn_readfirstlane(ld4(r, off)));
      if (v >= want) return v;
      if (fail(n)) return want;
      if (sleep) __builtin_amdgcn_s_sleep(2);
    }
  }
  __device__ __forceinline__ void wait_all(u32 off, int cnt, int lane) {
    for (u32 n = 0;;) {
      asm volatile("" ::: "memory");
      const u32 v = ld4(r, off + (lane < cnt ? lane : 0) * 128);
      if (__builtin_amdgcn_ballot_w64(v != enc(0)) == 0) return;
      if (fail(n)) return;
      __builtin_amdgcn_s_sleep(2);
    }
  }
};

#define D1_INIT 2
#define D2_INIT 57
#define PF_GAP 45
#define PF_SKIP 16384
#define QA_HOLD 450
#define F_HOLD 450
#define REF_LEAD 80
__device__ __forceinline__ u32 now32() { return (u32)__builtin_amdgcn_s_memrealtime(); }
__device__ __forceinline__ void wait_time(u32 until) {
  u32 left = until - now32();
  while (left - 6u < 495u) left = until - now32();
  if (left - 4u < 2u) asm volatile("s_nop 4");
  if (left == 5u) asm volatile("s_nop 4");
}
#define DPP0(v, ctrl, rows) ((u32)__builtin_amdgcn_update_dpp(0, (int)(v), (ctrl), (rows), 0xf, true))
__device__ __forceinline__ u32 wave_max_u32(u32 v) {
#define DPPM(c, r) v = __builtin_elementwise_max(v, DPP0(v, c, r))
  DPPM(0x111, 0xf); DPPM(0x112, 0xf); DPPM(0x114, 0xf); DPPM(0x118, 0xf); DPPM(0x142, 0xa); DPPM(0x143, 0xc);
  return (u32)__builtin_amdgcn_readlane((int)v, 63);
}
__device__ __forceinline__ float wave_sum_f32(float f) {
#define DPPF(c, r) f += __uint_as_float(DPP0(__float_as_uint(f), c, r))
  DPPF(0x111, 0xf); DPPF(0x112, 0xf); DPPF(0x114, 0xf); DPPF(0x118, 0xf); DPPF(0x142, 0xa); DPPF(0x143, 0xc);
  return __uint_as_float(__builtin_amdgcn_readlane(__float_as_uint(f), 63));
}
__device__ __forceinline__ void pace_wait(rsrc_t ws, u32 off, u32& ref, u32 d) {
#pragma unroll 1
  for (int k = 0; k < 16; ++k) {
    asm volatile("" ::: "memory");
    const u32 v = __builtin_amdgcn_readfirstlane(ld4(ws, off));
    if (v - ref - 1u < 0x7fffffffu && v - now32() + 400u < 800u) {
      ref = v;
      wait_time(v + d);
      return;
    }
    __builtin_amdgcn_s_sleep(2);
  }
  ref = now32() - d;
}
__device__ __forceinline__ void pace_adapt(u32& d, u32& hist, u32 misses, int s) {
  hist = ((hist << 1) | ((s > 2 && misses) ? 1u : 0u)) & 15u;
  const bool up = (hist & 1u) && (hist & 14u);
  d += up ? 2u : 0u;
  hist = up ? 0u : hist;
}

__device__ __forceinline__ int swz(int row) { return 2 * ((row & 1) | (((row >> 3) & 1) << 1) | ((((row >> 1) ^ (row >> 2)) & 1) << 2)); }

template <class F>
__device__ __forceinline__ void stage_chunk(ldsp slot, int w, int lane, F src) {
#pragma unroll
  for (int i = 0; i < 4; ++i) {
    const int row = 16 * w + 4 * i + (lane >> 4);
    const int c = (lane & 15) ^ swz(row);
    __builtin_amdgcn_global_load_lds((const GAS u32*)src(row, c), (LAS u32*)(slot + (16 * w + 4 * i) * 256), 16, 0, 0);
  }
}
__device__ __forceinline__ void touch(const u16* p, ldsp scratch) {
  __builtin_amdgcn_global_load_lds((const GAS u32*)p, (LAS u32*)scratch, 4, 0, 0);
}
__device__ __forceinline__ bf16x4 trd(ldsp p) { return __builtin_amdgcn_ds_read_tr16_b64_v4bf16((LAS bf16x4*)p); }
__device__ __forceinline__ int tr_base_dense(int tile, int lane) {
  const int g = lane >> 4, q = (lane >> 2) & 3, p = lane & 3, row = 8 * g + q;
  return row * 256 + (((2 * tile + (p >> 1)) ^ swz(row)) << 4) + ((p & 1) << 3);
}
__device__ __forceinline__ bf16x8 ext_dense(ldsp slot, int base, int ks) {
  const bf16x4 v0 = trd(slot + base + ks * 8192), v1 = trd(slot + (base ^ 128) + 1024 + ks * 8192);
  return __builtin_shufflevector(v0, v1, 0, 1, 2, 3, 4, 5, 6, 7);
}
__device__ __forceinline__ int tr_base_sparse(int tile, int lane) {
  const int g = lane >> 4, q = (lane >> 2) & 3, p = lane & 3, row = 16 * (g >> 1) + 4 * (g & 1) + (q & 1) + 8 * (q >> 1);
  return row * 256 + (((2 * tile + (p >> 1)) ^ swz(row)) << 4) + ((p & 1) << 3);
}
__device__ __forceinline__ bf16x16 ext_sparse(ldsp slot, int base, int t) {
  const bf16x4 v0 = trd(slot + base + t * 16384), v1 = trd(slot + (base ^ 128) + 512 + t * 16384);
  const bf16x4 v2 = trd(slot + base + 8192 + t * 16384), v3 = trd(slot + (base ^ 128) + 8704 + t * 16384);
  const bf16x8 a = __builtin_shufflevector(v0, v1, 0, 1, 2, 3, 4, 5, 6, 7);
  const bf16x8 b = __builtin_shufflevector(v2, v3, 0, 1, 2, 3, 4, 5, 6, 7);
  return __builtin_shufflevector(a, b, 0, 1, 2, 3, 4, 5, 6, 7, 8, 9, 10, 11, 12, 13, 14, 15);
}
template <int N> __device__ __forceinline__ void wait_vm() { asm volatile("s_waitcnt vmcnt(%0)" ::"i"(N) : "memory"); }
template <int NC, int NS, int C, class I, class T>
__device__ __forceinline__ void ring_step(ldsp lds, I& issue, T& take) {
  if constexpr (C < NC) {
    constexpr int last = (C == 0 ? NS - 1 : C - 2 + NS) < NC - 1 ? (C == 0 ? NS - 1 : C - 2 + NS) : NC - 1;
    wait_vm<4 * (last - C)>();
    bar();
    take(std::integral_constant<int, C>{}, lds + (C % NS) * 32768);
    if constexpr (C >= 1 && C - 1 + NS < NC) issue(std::integral_constant<int, C - 1 + NS>{}, lds + ((C - 1) % NS) * 32768);
    ring_step<NC, NS, C + 1>(lds, issue, take);
  }
}
template <class I>
__device__ __forceinline__ void ring_open(ldsp lds, I& issue) {
  issue(std::integral_constant<int, 0>{}, lds);
  issue(std::integral_constant<int, 1>{}, lds + 32768);
  issue(std::integral_constant<int, 2>{}, lds + 65536);
  issue(std::integral_constant<int, 3>{}, lds + 98304);
}
template <int NC, int NS, class I, class T>
__device__ __forceinline__ void ring_run(ldsp lds, I issue, T take, bool opened = false) {
  if (!opened) ring_open(lds, issue);
  if constexpr (NS == 5) issue(std::integral_constant<int, 4>{}, lds + 131072);
  ring_step<NC, NS, 0>(lds, issue, take);
  bar();
}

#define MFMA32(a, b, c) __builtin_amdgcn_mfma_f32_16x16x32_bf16((a), (b), (c), 0, 0, 0)
#define MFMA16(a, b, c) __builtin_amdgcn_mfma_f32_16x16x16bf16_1k(__builtin_bit_cast(s16x4, (a)), __builtin_bit_cast(s16x4, (b)), (c), 0, 0, 0)
#define CAT(x, y) __builtin_shufflevector(x, y, 0, 1, 2, 3, 4, 5, 6, 7, 8, 9, 10, 11, 12, 13, 14, 15)
#define SMF(a, b, c, idx) __builtin_amdgcn_smfmac_f32_16x16x64_bf16((a), (b), (c), (idx), 0, 0)

constexpr float SCALE = 0.08838834764831845f;
constexpr float LOG2E = 1.4426950408889634f;

constexpr int C_MEM = 0, C_O = 32768, C_Q = 8192, C_KM = 10240, C_VM = 12288, C_WO = 16384, C_KV = 16384, C_WV = 81920;
#define KVDELAY 7
#define ADELAY 2

__device__ __forceinline__ float vmax(float a, float b) { return __builtin_elementwise_maximum(a, b); }
__device__ __forceinline__ float vmax3(float a, float b, float c) { return vmax(vmax(a, b), c); }
__device__ __forceinline__ float xmax2(float a) {
  auto r = __builtin_amdgcn_permlane16_swap(__float_as_uint(a), __float_as_uint(a), false, false);
  a = vmax(__uint_as_float(r[0]), __uint_as_float(r[1]));
  auto q = __builtin_amdgcn_permlane32_swap(__float_as_uint(a), __float_as_uint(a), false, false);
  return vmax(__uint_as_float(q[0]), __uint_as_float(q[1]));
}
#define SCHED() __builtin_amdgcn_sched_barrier(0)
__device__ __forceinline__ f32x4 zero4() {
  f32x2 a, b;
  asm volatile("v_mov_b64 %0, 0" : "=v"(a));
  asm volatile("v_mov_b64 %0, 0\n\ts_nop 1" : "=v"(b));
  return __builtin_shufflevector(a, b, 0, 1, 2, 3);
}

__device__ __forceinline__ void put_tiles(ldsp dst, f32x4 t0, f32x4 t1) {
  const u32 v0 = pk2(swadd32(t0[0], t1[0]), swadd32(t0[1], t1[1])), v1 = pk2(swadd32(t0[2], t1[2]), swadd32(t0[3], t1[3]));
  *(LAS u16*)(dst) = (u16)v0;
  *(LAS u16*)(dst + 16) = (u16)(v0 >> 16);
  *(LAS u16*)(dst + 32) = (u16)v1;
  *(LAS u16*)(dst + 48) = (u16)(v1 >> 16);
}

template <int cc>
__device__ __forceinline__ void chain_stage(const Params& P, int h, int c, ldsp slot, int w, int lane) {
  if constexpr (cc >= 12) {
    const u16* base = P.wo + (size_t)h * DH * DM + c * 256 + (cc - 12) * 128;
    stage_chunk(slot, w, lane, [&](int row, int ch) { return base + row * DM + ch * 8; });
  } else if constexpr (cc >= 8) {
    const u16* base = P.wv + (size_t)(cc & 3) * 128 * WP + h * DH;
    stage_chunk(slot, w, lane, [&](int row, int ch) { return base + (size_t)row * WP + ch * 8; });
  } else {
    stage_chunk(slot, w, lane, [&](int row, int ch) { return (w < 4 ? P.wq : P.wk) + (size_t)(cc * 64 + (row & 63)) * WP + h * DH + ch * 8; });
  }
}

__device__ __forceinline__ void role_chain(const Params& P, rsrc_t ws, bool opened, ldsp lds, int h, int c, int tid) {
  const int lane = tid & 63, w = __builtin_amdgcn_readfirstlane(tid >> 6);
  Spin sp{ws, 0};
  bf16x16 w0[8], w1[8], wv[8];
  {
    bf16x16 wo[4];
    const int tb = tr_base_sparse(w, lane), tb0 = tr_base_sparse(w & 3, lane);
    auto issue = [&](auto C, ldsp slot) { chain_stage<C.value>(P, h, c, slot, w, lane); };
    auto take = [&](auto C, ldsp slot) {
      constexpr int cc = C.value;
      if constexpr (cc < 8) {
        ldsp half = slot + (w >> 2) * 16384;
        w0[cc] = ext_sparse(half, tb0, 0); w1[cc] = ext_sparse(half, tb0 ^ 128, 0);
      } else {
        const bf16x16 a = ext_sparse(slot, tb, 0), b = ext_sparse(slot, tb, 1);
        if constexpr (cc < 12) { wv[2 * (cc - 8)] = a; wv[2 * (cc - 8) + 1] = b; }
        else { wo[2 * (cc - 12)] = a; wo[2 * (cc - 12) + 1] = b; }
      }
    };
    ring_run<14, 5>(lds, issue, take, opened);
#pragma unroll
    for (int f = 0; f < 4; ++f) {
      ldsp dst = lds + C_WO + w * 8192 + f * 2048 + lane * 16;
      *(LAS bf16x8*)(dst) = __builtin_shufflevector(wo[f], wo[f], 0, 1, 2, 3, 4, 5, 6, 7);
      *(LAS bf16x8*)(dst + 1024) = __builtin_shufflevector(wo[f], wo[f], 8, 9, 10, 11, 12, 13, 14, 15);
    }
#pragma unroll
    for (int f = 0; f < 8; ++f) {
      if (w < 4 || f >= 6) {
        ldsp dst = lds + C_WV + (w & 3) * 20480 + (w < 4 ? f : f + 2) * 2048 + lane * 16;
        *(LAS bf16x8*)(dst) = __builtin_shufflevector(wv[f], wv[f], 0, 1, 2, 3, 4, 5, 6, 7);
        *(LAS bf16x8*)(dst + 1024) = __builtin_shufflevector(wv[f], wv[f], 8, 9, 10, 11, 12, 13, 14, 15);
      }
    }
    *(LAS u32x2*)(lds + C_VM + tid * 8) = (u32x2){0, 0};
  }
  const int psel = sp.wait_flag(OFF_PSEL, 0, 1);
  const u32 memoff = OFF_MEMR + (psel & 1) * 8192 + (w & 3) * 2048;
  const int g = lane >> 4, n = lane & 15;
  u32 partoff;
  {
    const int d = 256 * c + 128 * (lane >> 5) + 16 * w + n;
    partoff = OFF_PART + (d >> 4) * 16384 + ((psel >> 1) & 1) * 8192 + ((d >> 3) & 1) * 4096 + h * 128 + (d & 7) * 16 + (g & 1) * 8;
  }
  const int sidx = (n >> 3) ? 0xEEEE : 0x4444;
  const u32 l16 = lane * 16;
  ldsp qdst = lds + (w < 4 ? C_Q : C_KM) + (2 * (w & 3) + 8 * (g >> 1) + (n >> 3)) * 128 + (g & 1) * 64 + (n & 7) * 2;
  const f32x4 zero = {0, 0, 0, 0};

#define CHAIN_ADDR() \
    u32 lo_ = l16; asm volatile("" : "+v"(lo_));     \
    ldsp hot = lds + lo_;
#define CHAIN_TAIL() \
    { \
      const bf16x8 a0 = lds8(hot + C_O), a1 = lds8(hot + C_O + 1024); \
      pa = SMF(a0, CAT(wo[0], wo[1]), pa, sidx); pb = SMF(a0, CAT(wo[4], wo[5]), pb, sidx); \
      pa = SMF(a1, CAT(wo[2], wo[3]), pa, sidx); pb = SMF(a1, CAT(wo[6], wo[7]), pb, sidx); \
      u32x2 pv; \
      pv.x = nosent(pk2(swadd32(pa[0], pb[0]), swadd32(pa[1], pb[1]))); \
      pv.y = nosent(pk2(swadd32(pa[2], pb[2]), swadd32(pa[3], pb[3]))); \
      __builtin_amdgcn_raw_buffer_store_b64(pv, ws, partoff, (s & 1) * PART_SLOT, 16); \
      __builtin_amdgcn_s_setprio(0); \
    }

  u32 pref = now32() - 100000u, pd = D2_INIT, pfail = 0;
  if (w < 4) {
    const float mmask = g < 2 ? 1.0f : 0.0f;
    ldsp qsrc = lds + C_Q + g * 128 + (n & 7) * 16;
    ldsp odst = lds + C_O + (2 * w + (g >> 1)) * 128 + (n & 7) * 16 + (g & 1) * 8;
    u32 dur = 120;
    bf16x8 wo[8];
#pragma unroll
    for (int f = 0; f < 8; ++f) wo[f] = lds8(lds + C_WO + w * 8192 + l16 + f * 1024);
    int fl = -1;
    auto fetch_kv = [&](int s) {
      {
        if (sp.dec(__builtin_amdgcn_readlane(fl, 0)) < s) sp.wait_flag(OFF_FKV + h * 128, s, 1);
        if (s >= 16 && sp.dec(__builtin_amdgcn_readlane(fl, 1)) < s - 16) sp.wait_flag(OFF_FQA + (2 * h + (s & 1)) * 128, s - 16, 1);
      }
      const u32 o = OFF_KV + (u32)(h * NSEG + s) * 16384 + w * 4096;
      u32x4 t[4];
#pragma unroll
      for (int i = 0; i < 4; ++i) t[i] = ld16s(ws, l16 + i * 1024, o);
      fl = (int)ld4(ws, lane == 0 ? OFF_FKV + h * 128 : OFF_FQA + (2 * h + ((s + 1) & 1)) * 128);
#pragma unroll
      for (int i = 0; i < 4; ++i) *(LAS u32x4*)(lds + C_KV + w * 4096 + i * 1024 + l16) = t[i];
    };
    const bool pub = c == 0 && w == 3;
    auto publish_km = [&](int s) {
      const u32 o = OFF_KM + (u32)(h * NSEG + s) * 4096;
      const u32 vo = C_VM + l16 + (l16 & 0x300);
      u32x4 t0 = *(LAS u32x4*)(lds + C_KM + l16), t1 = *(LAS u32x4*)(lds + C_KM + 1024 + l16), t2 = *(LAS u32x4*)(lds + vo), t3 = *(LAS u32x4*)(lds + vo + 2048);
      st16s(ws, l16, o, nosent4(t0)); st16s(ws, l16 + 1024, o, nosent4(t1)); st16s(ws, l16 + 2048, o, nosent4(t2)); st16s(ws, l16 + 3072, o, nosent4(t3));
    };
    bar();
    fetch_kv(0);
    if (pub) { sp.wait_flag(OFF_FKM + h * 128, 0, 1); sp.wait_flag(OFF_FKM + h * 128 + 4, 0, 1); }
    for (int s = 0; s < NSEG - 1; ++s) {
      CHAIN_ADDR();
      bar();
      bf16x8 kx[8], vx0, vx1;
      u32 tstart;
      ldsp vmp = lds + C_VM + w * 512 + (lo_ >> 1);
      {
        tstart = now32();
        if (tid == 0) st4(ws, OFF_POST + (psel & 1) * 8192 + (2 * h + c) * 128, tstart + dur);
        __builtin_amdgcn_s_sleep(ADELAY);
        __builtin_amdgcn_s_waitcnt(0xC07F);
        bf16x8 a[4];
#pragma unroll
        for (int t = 0; t < 4; ++t) a[t] = lds8(hot + t * 1024);
#pragma unroll
        for (int i = 0; i < 8; ++i) kx[i] = lds8(hot + C_KV + i * 1024);
        vx0 = lds8(hot + C_KV + 8192 + w * 1024); vx1 = lds8(hot + C_KV + 12288 + w * 1024);
        __builtin_amdgcn_s_setprio(2);
        f32x4 q0 = zero4(), q1 = zero4();
        SCHED();
#pragma unroll
        for (int t = 0; t < 8; ++t) {
          q0 = SMF(a[t & 3], w0[t], q0, sidx); q1 = SMF(a[t & 3], w1[t], q1, sidx);
          asm volatile("" :: "v"(a[t & 3]) : "memory");
          if (t < 4) a[t] = lds8(hot + (t + 4) * 1024);
          SCHED();
        }
        put_tiles(qdst, q0, q1);
      }
      bar();
      bf16x8 bq[4], km[4];
#pragma unroll
      for (int ks = 0; ks < 4; ++ks) { bq[ks] = lds8(qsrc + ks * 512); km[ks] = lds8(qsrc + (C_KM - C_Q) + ks * 512); }
      SCHED();
      f32x4 sm = zero, s0 = zero, s1 = zero;
#pragma unroll
      for (int ks = 0; ks < 4; ++ks) {
        s0 = MFMA32(kx[ks], bq[ks], s0);
        s1 = MFMA32(kx[4 + ks], bq[ks], s1);
        sm = MFMA32(km[ks], bq[ks], sm);
      }
      const float mx0 = vmax3(vmax3(s0[0], s0[1], s0[2]), vmax3(s0[3], s1[0], s1[1]), vmax(s1[2], s1[3]));
      const float mx = xmax2(vmax3(vmax3(mx0, sm[0], sm[1]), sm[2], sm[3]));
      constexpr float CE = SCALE * LOG2E;
      const float nb = -mx * CE;
#pragma unroll
      for (int r = 0; r < 4; ++r) {
        s0[r] = __builtin_amdgcn_exp2f(fmaf(s0[r], CE, nb));
        s1[r] = __builtin_amdgcn_exp2f(fmaf(s1[r], CE, nb));
      }
      const u32x4 px = {pk2(s0[0], s0[1]), pk2(s0[2], s0[3]), pk2(s1[0], s1[1]), pk2(s1[2], s1[3])};
      f32x4 ox0 = MFMA32(vx0, __builtin_bit_cast(bf16x8, px), zero);
      f32x4 ox1 = MFMA32(vx1, __builtin_bit_cast(bf16x8, px), zero);
#pragma unroll
      for (int r = 0; r < 4; ++r) sm[r] = __builtin_amdgcn_exp2f(fmaf(sm[r], CE, nb));
      u32x2 pm = {pk2(sm[0], sm[1]), pk2(sm[2], sm[3])};
      asm volatile("" : "+v"(pm), "+v"(ox0), "+v"(ox1));
      bar();
      const u32x2 vm0 = *(LAS u32x2*)(vmp), vm1 = *(LAS u32x2*)(vmp + 2048);
      SCHED();
      const float sum = fmaf(((sm[0] + sm[1]) + (sm[2] + sm[3])), mmask, ((s0[0] + s0[1]) + (s0[2] + s0[3])) + ((s1[0] + s1[1]) + (s1[2] + s1[3])));
      float inv = __builtin_amdgcn_rcpf(xsum(sum));
      asm volatile("" : "+v"(inv));
      f32x4 om0 = MFMA16(vm0, pm, zero);
      f32x4 om1 = MFMA16(vm1, pm, zero);
      asm volatile("" : "+v"(om0), "+v"(om1));
      const f32x4 o0 = (ox0 + om0) * inv, o1 = (ox1 + om1) * inv;
      *(LAS u32x2*)(odst) = (u32x2){pk2(o0[0], o0[1]), pk2(o0[2], o0[3])};
      *(LAS u32x2*)(odst + 1024) = (u32x2){pk2(o1[0], o1[1]), pk2(o1[2], o1[3])};
      f32x4 pa = zero4(), pb = zero4();
      bar();
      CHAIN_TAIL();
      dur = now32() - tstart;
      {
        __builtin_amdgcn_s_sleep(KVDELAY);
        if (pub) publish_km(s);
        fetch_kv(s + 1);
      }
    }
    {
      bar();
      bar();
      bar();
      if (pub) publish_km(NSEG - 1);
    }
  } else {
    ldsp vdst = lds + C_VM + (lane < 32 ? w : w - 4) * 512 + (lane & 31) * 8;
    bar();
    for (int s = 0; s < NSEG; ++s) {
      CHAIN_ADDR();
      u32 nn = 0;
      {
        u32x4 m0, m1;
        if (s > 0) pace_wait(ws, OFF_REF + (psel & 1) * 8192, pref, pd);
        for (;;) {
          asm volatile("" ::: "memory");
          m0 = ld16s(ws, lo_, memoff + (s & 3) * 16384);
          m1 = ld16s(ws, lo_ + 1024, memoff + (s & 3) * 16384);
          *(LAS u32x4*)(hot + (w - 4) * 2048) = m0;
          *(LAS u32x4*)(hot + (w - 4) * 2048 + 1024) = m1;
          if (!any_sent(umax4(m0, m1))) break;
          if (sp.fail(nn)) break;
        }
      }
      bar();
      bf16x8 a[8];
#pragma unroll
      for (int t = 0; t < 8; ++t) a[t] = lds8(hot + t * 1024);
      __builtin_amdgcn_s_setprio(3);
      SCHED();
      f32x4 va = zero, vb = zero;
#pragma unroll
      for (int t = 0; t < 8; ++t) { va = SMF(a[t], w0[t], va, sidx); vb = SMF(a[t], w1[t], vb, sidx); }
      SCHED();
      put_tiles(qdst, va, vb);
      va = zero; vb = zero;
      {
        ldsp wvp = hot + C_WV + (w - 4) * 20480;
        bf16x8 p0[10], p1[10];
#define LDP(i) { p0[i] = lds8(wvp + (i) * 2048); p1[i] = lds8(wvp + (i) * 2048 + 1024); }
#define VB(t) { SCHED(); vb = SMF(a[t], CAT(p0[t], p1[t]), vb, sidx); SCHED(); }
#define VA(t) { va = SMF(a[t], wv[t], va, sidx); }
        LDP(0); LDP(1);
        asm volatile("" :: "v"(CAT(a[0], a[1])), "v"(CAT(a[2], a[3])));
        __builtin_amdgcn_s_setprio(0);
        SCHED(); VA(0); VA(1);
        VB(0); LDP(2);
        VB(1); LDP(3); LDP(4);
        SCHED(); VA(2); VA(3);
        asm volatile("" ::: "memory");
        __builtin_amdgcn_s_waitcnt(0xC67F);
        rawbar();
        VB(2); LDP(5);
        VB(3); LDP(6); LDP(7);
        __builtin_amdgcn_s_setprio(3);
        SCHED(); VA(4); VA(5);
        VB(4); LDP(8);
        VB(5); LDP(9);
        VB(6); VB(7);
        va = SMF(a[6], CAT(p0[8], p1[8]), va, sidx);
        va = SMF(a[7], CAT(p0[9], p1[9]), va, sidx);
      }
      __builtin_amdgcn_s_setprio(3);
      u32x2 vm;
      vm.x = pk2(swadd32(va[0], vb[0]), swadd32(va[1], vb[1]));
      vm.y = pk2(swadd32(va[2], vb[2]), swadd32(va[3], vb[3]));
      *(LAS u32x2*)(vdst) = vm;
      __builtin_amdgcn_s_setprio(0);
      bar();
      if (s < NSEG - 1) {
        bf16x8 wo[8];
#pragma unroll
        for (int f = 0; f < 8; ++f) wo[f] = lds8(hot + C_WO + w * 8192 + f * 1024);
        f32x4 pa = zero, pb = zero;
        asm volatile("" : "+v"(pa), "+v"(pb));
        __builtin_amdgcn_s_setprio(3);
        bar();
        CHAIN_TAIL();
        pace_adapt(pd, pfail, nn, s);
      }
    }
  }
}

__device__ __forceinline__ void bulk_prefetch(const Params& P, int p, ldsp scratch, int tid) {
  const int w = __builtin_amdgcn_readfirstlane(tid >> 6);
  if (w < 4) return;
  const u32 t0 = now32();
#pragma unroll
  for (int k = 0; k < 4; ++k) {
    const int i = w - 4 + 4 * k;
    wait_time(t0 + i * PF_GAP);
    const u32 L0 = PF_SKIP + (u32)(i * 160 + p) * 64;
    if (L0 >= 131072u) break;
    const u16* base = L0 < 65536u ? (((L0 >> 12) & 1) ? P.wk : P.wq) + (size_t)(L0 >> 13) * 262144 + (L0 & 4095u) * 64
                    : L0 < 98304u ? P.wv + (size_t)(L0 - 65536u) * 64 : P.wo + (size_t)(L0 - 98304u) * 64;
    touch(base + (tid & 63) * 64, scratch + w * 256);
  }
}

__device__ __forceinline__ void role_reduce(const Params& P, rsrc_t ws, int r, int tid) {
  const int lane = tid & 63, w = __builtin_amdgcn_readfirstlane(tid >> 6);
  Spin sp{ws, 0};
  if (r == 0 && w == 2) {
    u32 best[4];
#pragma unroll
    for (int k = 0; k < 4; ++k) best[k] = 0xFFFFFFFFu;
    for (int rep = 0; rep < 4; ++rep) {
#pragma unroll
      for (int k = 0; k < 4; ++k) {
        const u32 off = ((k & 2) ? OFF_PART : OFF_PROBE) + (k & 1) * 8192;
        const unsigned long long t0 = __builtin_amdgcn_s_memtime();
        asm volatile("s_waitcnt lgkmcnt(0)" ::: "memory");
        if (lane == 0) st4(ws, off, SENT);
        drain();
        const unsigned long long t1 = __builtin_amdgcn_s_memtime();
        asm volatile("s_waitcnt lgkmcnt(0)" ::: "memory");
        const u32 d = (u32)(t1 - t0);
        best[k] = d < best[k] ? d : best[k];
      }
    }
    const u32 sel = (best[1] < best[0] ? 1u : 0u) | (best[3] < best[2] ? 2u : 0u);
    if (lane == 0) st4(ws, OFF_PSEL, sp.enc((int)sel));
  }
  if (w >= 2) return;
  const int u = 2 * r + w;
  const int psel = sp.wait_flag(OFF_PSEL, 0, 1);
  const u32 inbox = OFF_PART + (u >> 1) * 16384 + ((psel >> 1) & 1) * 8192 + (u & 1) * 4096;
  const u32 l16 = lane * 16;
  const u32x4 sent = {SENT, SENT, SENT, SENT};
#pragma unroll
  for (int i = 0; i < 4; ++i) { st16s(ws, l16 + i * 1024, inbox, sent); st16s(ws, l16 + i * 1024, inbox + PART_SLOT, sent); }
  const int n = lane & 15, g = lane >> 4;
  const bool act = n < 8;
  const int col = 4 * (g & 1) + 2 * (g >> 1);
  const u32 memoff = OFF_MEMR + (psel & 1) * 8192 + u * 128 + (n & 7) * 16 + col * 2;
  const u32 memst = act ? memoff : 0x80000000u;
  const u32 m0 = nosent(*(const u32*)(P.mem0 + (n & 7) * DM + 8 * u + col));
  drain();
  if (act) st4(ws, memoff, m0);
  u32x4 selv;
#pragma unroll
  for (int k = 0; k < 4; ++k) selv[k] = (n == 2 * k ? 0x3F80u : 0u) | (n == 2 * k + 1 ? 0x3F800000u : 0u);
  const bf16x8 selb = __builtin_bit_cast(bf16x8, selv);
  u32 pref = now32() - 100000u, pd = D1_INIT, pfail = 0;
  for (int s = 0; s < NSEG - 1; ++s) {
    f32x4 acc; u32 nn = 0, y;
    const u32 in = inbox + (s & 1) * PART_SLOT;
    u32 outslot = ((s + 1) & 3) * 16384;
    asm volatile("" : "+s"(outslot));
    {
      wait_time(pref + REF_LEAD);
#pragma unroll 1
      for (int k = 0; k < (s ? 12 : 64); ++k) {
        asm volatile("" ::: "memory");
        const u32 v = ld4(ws, OFF_POST + (psel & 1) * 8192 + lane * 128), e = v - pref;
        if (__builtin_amdgcn_ballot_w64(e - 1u >= 0x7fffffffu || v - now32() + 600u >= 1000u) == 0) {
          const u32 ref = pref + wave_max_u32(e);
          if (u == 0 && lane == 0) st4(ws, OFF_REF + (psel & 1) * 8192, ref);
          wait_time(ref + pd);
          break;
        }
        __builtin_amdgcn_s_sleep(1);
      }
    }
    for (;;) {
      asm volatile("" ::: "memory");
      u32x4 v[4];
#pragma unroll
      for (int i = 0; i < 4; ++i) v[i] = ld16s(ws, l16 + i * 1024, in);
      SCHED();
      acc = (f32x4){0, 0, 0, 0};
#pragma unroll
      for (int i = 0; i < 4; ++i) acc = MFMA32(__builtin_bit_cast(bf16x8, v[i]), selb, acc);
      y = nosent(pk2(swadd32(acc[0], acc[2]), swadd32(acc[1], acc[3])));
      if (__builtin_amdgcn_ballot_w64(__builtin_isunordered(bflo(y), bfhi(y))) == 0) break;
      if (!(any_sent(v[0]) | any_sent(v[1]) | any_sent(v[2]) | any_sent(v[3]))) break;
      if (sp.fail(nn)) break;
    }
    __builtin_amdgcn_raw_buffer_store_b32(y, ws, memst, outslot, 16);
    pace_adapt(pd, pfail, nn, s);
    pref = now32();
    __builtin_amdgcn_raw_buffer_store_b32(SENT, ws, memst, ((s + 3) & 3) * 16384, 16);
#pragma unroll
    for (int i = 0; i < 4; ++i) st16s(ws, l16 + i * 1024, in, sent);
  }
}

__device__ __forceinline__ void stage_x(const u16* x, int s, ldsp dst, int w, int lane) {
#pragma unroll
  for (int i = 0; i < 4; ++i) {
    const int f = 4 * w + i;
    const u16* src = x + (size_t)(32 * s + 16 * (f >> 4) + (lane & 15)) * DM + 32 * (f & 15) + 8 * (lane >> 4);
    __builtin_amdgcn_global_load_lds((const GAS u32*)src, (LAS u32*)(dst + f * 1024), 16, 0, 0);
  }
}

template <int cc>
__device__ __forceinline__ void kv_stage(const Params& P, int h, ldsp slot, int w, int lane) {
  const u16* base = (cc < 4 ? P.wk : P.wv) + (size_t)(cc & 3) * 128 * WP + h * DH;
  stage_chunk(slot, w, lane, [&](int row, int ch) { return base + (size_t)row * WP + ch * 8; });
}

__device__ __forceinline__ void role_kv(const Params& P, rsrc_t ws, bool opened, ldsp lds, int h, int tid) {
  const int lane = tid & 63, w = __builtin_amdgcn_readfirstlane(tid >> 6);
  const Spin sp{ws, 0};
  bf16x8 wk[16], wv[16];
  const int tb = tr_base_dense(w, lane);
  stage_x(P.x, 0, lds + 131072, w, lane);
  {
    auto issue = [&](auto C, ldsp slot) { kv_stage<C.value>(P, h, slot, w, lane); };
    auto take = [&](auto C, ldsp slot) {
      constexpr int cc = C.value;
#pragma unroll
      for (int ks = 0; ks < 4; ++ks) {
        if constexpr (cc < 4) wk[4 * cc + ks] = ext_dense(slot, tb, ks);
        else wv[4 * (cc - 4) + ks] = ext_dense(slot, tb, ks);
      }
    };
    ring_run<8, 4>(lds, issue, take, opened);
  }
  const int g = lane >> 4, n = lane & 15;
  for (int s = 0; s < NSEG; ++s) {
    ldsp xb = lds + ((s & 1) ? 0 : 131072);
    drain();
    bar();
    if (s > 0 && tid == 0) st4(ws, OFF_FKV + h * 128, sp.enc(s - 1));
    if (s + 1 < NSEG) stage_x(P.x, s + 1, lds + ((s & 1) ? 131072 : 0), w, lane);
    f32x4 k0 = {0, 0, 0, 0}, k1 = k0, v0 = k0, v1 = k0;
#pragma unroll
    for (int ks = 0; ks < 16; ++ks) {
      const bf16x8 x0 = lds8(xb + ks * 1024 + lane * 16), x1 = lds8(xb + 16384 + ks * 1024 + lane * 16);
      k0 = MFMA32(wk[ks], x0, k0); k1 = MFMA32(wk[ks], x1, k1);
      v0 = MFMA32(x0, wv[ks], v0); v1 = MFMA32(x1, wv[ks], v1);
    }
    const u32 o = OFF_KV + (u32)(h * NSEG + s) * 16384;
    const u32 ko = o + (w >> 1) * 1024 + ((2 * (w & 1) + (g >> 1)) * 16 + n) * 16 + (g & 1) * 8;
    st8(ws, ko, (u32x2){pk2(k0[0], k0[1]), pk2(k0[2], k0[3])});
    st8(ws, ko + 4096, (u32x2){pk2(k1[0], k1[1]), pk2(k1[2], k1[3])});
    st16(ws, o + 8192 + w * 1024 + lane * 16, (u32x4){pk2(v0[0], v0[1]), pk2(v0[2], v0[3]), pk2(v1[0], v1[1]), pk2(v1[2], v1[3])});
  }
  drain();
  bar();
  if (tid == 0) st4(ws, OFF_FKV + h * 128, sp.enc(NSEG - 1));
}

constexpr int Q_X0 = 0, Q_X1 = 32768, Q_Q = 65536, Q_KX = 73728, Q_VX = 81920, Q_KM = 90112, Q_VM = 92160, Q_O = 94208;

__device__ __forceinline__ void role_qa(const Params& P, rsrc_t ws, u32 tin, ldsp lds, int h, int par, int tid) {
  const int lane = tid & 63, w = __builtin_amdgcn_readfirstlane(tid >> 6);
  Spin sp{ws, 0};
  bf16x8 wq[16], wo[16];
  const int tb = tr_base_dense(w, lane);
  const u32x4 sent = {SENT, SENT, SENT, SENT};
  if (w == 0) {
#pragma unroll
    for (int i = 0; i < 32; ++i) st16s(ws, lane * 16 + (i & 3) * 1024, OFF_KM + (u32)(h * NSEG + 2 * (i >> 2) + par) * 4096, sent);
    drain();
    if (lane == 0) st4(ws, OFF_FKM + h * 128 + par * 4, sp.enc(0));
  }
  wait_time(tin + 450); wait_time(tin + QA_HOLD);
  {
    auto issue = [&](auto C, ldsp slot) {
      constexpr int cc = C.value;
      if constexpr (cc < 4) {
        const u16* base = P.wq + (size_t)cc * 128 * WP + h * DH;
        stage_chunk(slot, w, lane, [&](int row, int ch) { return base + (size_t)row * WP + ch * 8; });
      } else {
        const u16* base = P.wo + (size_t)h * DH * DM + (cc - 4) * 128;
        stage_chunk(slot, w, lane, [&](int row, int ch) { return base + row * DM + ch * 8; });
      }
    };
    auto take = [&](auto C, ldsp slot) {
      constexpr int cc = C.value;
#pragma unroll
      for (int ks = 0; ks < 4; ++ks) {
        if constexpr (cc < 4) wq[4 * cc + ks] = ext_dense(slot, tb, ks);
        else wo[4 * (cc - 4) + ks] = ext_dense(slot, tb, ks);
      }
    };
    ring_run<8, 4>(lds, issue, take);
  }
  const int g = lane >> 4, n = lane & 15;
  const int qt = w & 1, ep = w >> 1;
  const int img = (w >> 1) * 1024 + ((2 * (w & 1) + (g >> 1)) * 16 + n) * 16 + (g & 1) * 8;
  int kvseen = -1;
  const float mmask = g < 2 ? 1.0f : 0.0f;
  const f32x4 zero = {0, 0, 0, 0};
  const int qi = 16 * qt + n;
  u32x4 kva, kvb;
  auto kv_load = [&](int s) {
    if (kvseen < s) kvseen = sp.wait_flag(OFF_FKV + h * 128, s, 1);
    const u32 kvo = OFF_KV + (u32)(h * NSEG + s) * 16384;
    kva = ld16s(ws, tid * 16, kvo); kvb = ld16s(ws, tid * 16, kvo + 8192);
  };
  kv_load(par);
  stage_x(P.x, par, lds + Q_X0, w, lane);
  for (int it = 0; it < NSEG / 2; ++it) {
    const int s = 2 * it + par;
    ldsp xb = lds + ((it & 1) ? Q_X1 : Q_X0);
    drain();
    bar();
    if (it > 0 && tid == 0) st4(ws, OFF_FQA + (2 * h + par) * 128, sp.enc(s - 2));
    const u32 kmo = OFF_KM + (u32)(h * NSEG + s) * 4096;
    const u32x4 kme = ld16s(ws, (tid & 255) * 16, kmo);
    {
      f32x4 q0 = zero, q1 = zero;
#pragma unroll
      for (int ks = 0; ks < 16; ++ks) {
        q0 = MFMA32(wq[ks], lds8(xb + ks * 1024 + lane * 16), q0);
        q1 = MFMA32(wq[ks], lds8(xb + 16384 + ks * 1024 + lane * 16), q1);
      }
      *(LAS u32x2*)(lds + Q_Q + img) = (u32x2){pk2(q0[0], q0[1]), pk2(q0[2], q0[3])};
      *(LAS u32x2*)(lds + Q_Q + 4096 + img) = (u32x2){pk2(q1[0], q1[1]), pk2(q1[2], q1[3])};
    }
    *(LAS u32x4*)(lds + Q_KX + tid * 16) = kva;
    *(LAS u32x4*)(lds + Q_VX + tid * 16) = kvb;
    if (w < 4) *(LAS u32x4*)(lds + Q_KM + tid * 16) = kme;
    const bool kmok = !any_sent(kme);
    bar();
    if (it + 1 < NSEG / 2) kv_load(s + 2);
    if (w >= 4 && s + 16 < NSEG) st16s(ws, (tid & 255) * 16, kmo + 16 * 4096, sent);
    if (it + 1 < NSEG / 2) stage_x(P.x, s + 2, lds + ((it & 1) ? Q_X0 : Q_X1), w, lane);
    bf16x8 bq[4];
    f32x4 s0 = zero, s1 = zero, sm = zero;
#pragma unroll
    for (int ks = 0; ks < 4; ++ks) {
      bq[ks] = lds8(lds + Q_Q + qt * 4096 + ks * 1024 + lane * 16);
      s0 = MFMA32(lds8(lds + Q_KX + ks * 1024 + lane * 16), bq[ks], s0);
      s1 = MFMA32(lds8(lds + Q_KX + 4096 + ks * 1024 + lane * 16), bq[ks], s1);
    }
#pragma unroll
    for (int r = 0; r < 4; ++r) {
      s0[r] = (4 * g + r <= qi) ? s0[r] : -INFINITY;
      s1[r] = (16 + 4 * g + r <= qi) ? s1[r] : -INFINITY;
    }
    float mx0 = vmax3(vmax3(s0[0], s0[1], s0[2]), vmax3(s0[3], s1[0], s1[1]), vmax(s1[2], s1[3]));
    const bf16x8 vx0 = lds8(lds + Q_VX + 2 * ep * 1024 + lane * 16), vx1 = lds8(lds + Q_VX + (2 * ep + 1) * 1024 + lane * 16);
    asm volatile("" : "+v"(mx0));
    if (w < 4) {
      if (!kmok) {
        u32x4 kmv;
        for (u32 nn = 0;;) {
          asm volatile("" ::: "memory");
          kmv = ld16s(ws, tid * 16, kmo);
          if (!any_sent(kmv) || sp.fail(nn)) break;
          __builtin_amdgcn_s_sleep(1);
        }
        *(LAS u32x4*)(lds + Q_KM + tid * 16) = kmv;
      }
    } else if (it == 16 && w == 4) {
      sp.wait_all(OFF_FY, 32, lane);
    }
    bar();
    {
      const int mo = g * 128 + (n & 7) * 16;
#pragma unroll
      for (int ks = 0; ks < 4; ++ks) sm = MFMA32(lds8(lds + Q_KM + ks * 512 + mo), bq[ks], sm);
      u32x2 vm0 = *(LAS u32x2*)(lds + Q_VM + 2 * ep * 256 + (lane & 31) * 8), vm1 = *(LAS u32x2*)(lds + Q_VM + (2 * ep + 1) * 256 + (lane & 31) * 8);
      if (g >= 2) { vm0 = (u32x2){0, 0}; vm1 = vm0; }
      const float mx = xmax2(vmax3(vmax3(mx0, sm[0], sm[1]), sm[2], sm[3]));
      constexpr float CE = SCALE * LOG2E;
      const float nb = -mx * CE;
#pragma unroll
      for (int r = 0; r < 4; ++r) {
        s0[r] = __builtin_amdgcn_exp2f(fmaf(s0[r], CE, nb));
        s1[r] = __builtin_amdgcn_exp2f(fmaf(s1[r], CE, nb));
        sm[r] = __builtin_amdgcn_exp2f(fmaf(sm[r], CE, nb));
      }
      const u32x4 px = {pk2(s0[0], s0[1]), pk2(s0[2], s0[3]), pk2(s1[0], s1[1]), pk2(s1[2], s1[3])};
      const u32x2 pm = {pk2(sm[0], sm[1]), pk2(sm[2], sm[3])};
      const f32x4 ox0 = MFMA32(vx0, __builtin_bit_cast(bf16x8, px), zero), ox1 = MFMA32(vx1, __builtin_bit_cast(bf16x8, px), zero);
      const f32x4 om0 = MFMA16(vm0, pm, zero), om1 = MFMA16(vm1, pm, zero);
      const float sum = fmaf(((sm[0] + sm[1]) + (sm[2] + sm[3])), mmask, ((s0[0] + s0[1]) + (s0[2] + s0[3])) + ((s1[0] + s1[1]) + (s1[2] + s1[3])));
      const float inv = __builtin_amdgcn_rcpf(xsum(sum));
      const f32x4 o0 = (ox0 + om0) * inv, o1 = (ox1 + om1) * inv;
      ldsp od = lds + Q_O + qt * 4096 + ep * 1024 + ((g >> 1) * 16 + n) * 16 + (g & 1) * 8;
      *(LAS u32x2*)(od) = (u32x2){pk2(o0[0], o0[1]), pk2(o0[2], o0[3])};
      *(LAS u32x2*)(od + 512) = (u32x2){pk2(o1[0], o1[1]), pk2(o1[2], o1[3])};
    }
    bar();
    {
      bf16x8 ob[8];
#pragma unroll
      for (int f = 0; f < 8; ++f) ob[f] = lds8(lds + Q_O + f * 1024 + lane * 16);
#pragma unroll
      for (int pn = 0; pn < 4; ++pn) {
        f32x4 a0 = zero, a1 = zero;
#pragma unroll
        for (int ks = 0; ks < 4; ++ks) { a0 = MFMA32(wo[4 * pn + ks], ob[ks], a0); a1 = MFMA32(wo[4 * pn + ks], ob[4 + ks], a1); }
        st16s(ws, lane * 16, OFF_XP + (u32)((s * 32 + 8 * pn + w) * 32 + h) * 1024,
              nosent4((u32x4){pk2(a0[0], a0[1]), pk2(a0[2], a0[3]), pk2(a1[0], a1[1]), pk2(a1[2], a1[3])}));
      }
    }
  }
}

constexpr int F_RG = 131072, F_TOP = 139264, F_U = 139776, F_H = 143872, F_BS = 145920, F_RED = 147456;

__device__ __forceinline__ void role_final(const Params& P, rsrc_t ws, u32 tin, ldsp lds, int j, int tid) {
  const int lane = tid & 63, w = __builtin_amdgcn_readfirstlane(tid >> 6);
  const int g = lane >> 4, n = lane & 15;
  Spin sp{ws, 0};
  wait_time(tin + 450); wait_time(tin + F_HOLD);
  {
    const u32x4 wr = *(const u32x4*)(P.wr + lane * 8), gw = *(const u32x4*)(P.gw + lane * 8);
    u32x4 xv[8];
#pragma unroll
    for (int i = 0; i < 8; ++i) xv[i] = *(const u32x4*)(P.x + (size_t)(64 * j + 8 * w + i) * DM + lane * 8);
    u32 mine = 0;
#pragma unroll
    for (int i = 0; i < 8; ++i) {
      float r = 0.f, z = 0.f;
#pragma unroll
      for (int k = 0; k < 4; ++k) {
        r = fmaf(bflo(xv[i][k]), bflo(wr[k]), r); r = fmaf(bfhi(xv[i][k]), bfhi(wr[k]), r);
        z += bfr(bflo(xv[i][k]) * bflo(gw[k])); z += bfr(bfhi(xv[i][k]) * bfhi(gw[k]));
      }
      r = wave_sum_f32(r); z = wave_sum_f32(z);
      const u32 v = pk2(r, sigmoidf_(bfr(z)));
      if (lane == i) mine = v;
    }
    if (lane < 8) st4(ws, OFF_RG + (64 * j + 8 * w + lane) * 4, mine);
    drain();
    bar();
    if (tid == 0) st4(ws, OFF_FY + j * 128 + 4, sp.enc(0));
  }
  bf16x8 wa[16], wd[8];
  const int tb = tr_base_dense(w, lane);
  if (j < 28) {
    auto issue = [&](auto C, ldsp slot) {
      constexpr int cc = C.value;
      if constexpr (cc < 4) {
        stage_chunk(slot, w, lane, [&](int row, int ch) { return (ch < 8 ? P.wg : P.wu) + (size_t)(cc * 128 + row) * FF + 64 * j + (ch & 7) * 8; });
      } else {
        stage_chunk(slot, w, lane, [&](int row, int ch) { return P.wd + (size_t)(64 * j + (row & 63)) * DM + (2 * (cc - 4) + (row >> 6)) * 128 + ch * 8; });
      }
    };
    auto take = [&](auto C, ldsp slot) {
      constexpr int cc = C.value;
#pragma unroll
      for (int ks = 0; ks < 4; ++ks) {
        if constexpr (cc < 4) wa[4 * cc + ks] = ext_dense(slot, tb, ks);
        else wd[4 * (cc - 4) + ks] = ext_dense(slot, tb, ks);
      }
    };
    ring_run<6, 4>(lds, issue, take);
  }
#pragma unroll
  for (int i = 0; i < 8; ++i) st16s(ws, tid * 16 + (i & 3) * 8192, OFF_XP + (u32)(((NSEG - 2 + (i >> 2)) * 32 + j) * 32) * 1024, (u32x4){SENT, SENT, SENT, SENT});
  u32 sel[8];
  {
    sp.wait_all(OFF_FY + 4, 32, lane);
    const u32x4 m = ld16(ws, OFF_RG + tid * 16);
    *(LAS u32x4*)(lds + F_RG + tid * 16) = m;
    u32 key[4];
#pragma unroll
    for (int k = 0; k < 4; ++k) {
      const u32 b = m[k] & 0xffffu;
      const u32 o = (b & 0x8000u) ? (~b & 0xffffu) : (b | 0x8000u);
      key[k] = (o << 16) | (u32)(0xffff - (tid * 4 + k));
    }
    u32 mine = 0;
#pragma unroll
    for (int r = 0; r < 8; ++r) {
      const u32 a = key[0] > key[1] ? key[0] : key[1], b = key[2] > key[3] ? key[2] : key[3];
      const u32 t = wave_max_u32(a > b ? a : b);
#pragma unroll
      for (int k = 0; k < 4; ++k) key[k] = key[k] == t ? 0u : key[k];
      if (lane == r) mine = t;
    }
    if (lane < 8) *(LAS u32*)(lds + F_TOP + (w * 8 + lane) * 4) = mine;
    bar();
    u32 k2 = *(LAS u32*)(lds + F_TOP + lane * 4);
#pragma unroll
    for (int r = 0; r < 8; ++r) {
      const u32 t = wave_max_u32(k2);
      k2 = k2 == t ? 0u : k2;
      sel[r] = __builtin_amdgcn_readfirstlane((0xffffu - (t & 0xffffu)) & 2047u);
    }
  }
  u32 myrow = sel[0];
#pragma unroll
  for (int r = 1; r < 8; ++r) myrow = (n & 7) == r ? sel[r] : myrow;
  if (j < 28) {
    f32x4 acc = {0, 0, 0, 0};
    bf16x8 xs[16];
#pragma unroll
    for (int ks = 0; ks < 16; ++ks) xs[ks] = *(const bf16x8*)(P.x + (size_t)myrow * DM + 32 * ks + 8 * g);
    SCHED();
#pragma unroll
    for (int ks = 0; ks < 16; ++ks) acc = MFMA32(wa[ks], xs[ks], acc);
    if (w >= 4) *(LAS f32x4*)(lds + F_U + (w - 4) * 1024 + lane * 16) = acc;
    bar();
    if (w < 4) {
      const f32x4 up = *(LAS f32x4*)(lds + F_U + w * 1024 + lane * 16);
      float hv[4];
#pragma unroll
      for (int r = 0; r < 4; ++r) { const float a = bfr(acc[r]); hv[r] = bfr(a * sigmoidf_(a)) * bfr(up[r]); }
      *(LAS u32x2*)(lds + F_H + (w >> 1) * 1024 + ((2 * (w & 1) + (g >> 1)) * 16 + n) * 16 + (g & 1) * 8) = (u32x2){pk2(hv[0], hv[1]), pk2(hv[2], hv[3])};
    }
    bar();
    const bf16x8 h0 = lds8(lds + F_H + lane * 16), h1 = lds8(lds + F_H + 1024 + lane * 16);
#pragma unroll
    for (int pn = 0; pn < 4; ++pn) {
      f32x4 y = {0, 0, 0, 0};
      y = MFMA32(wd[2 * pn], h0, y); y = MFMA32(wd[2 * pn + 1], h1, y);
      if (n < 8) st16(ws, OFF_YP + (u32)((8 * pn + w) * 28 + j) * 512 + (n * 4 + g) * 16, __builtin_bit_cast(u32x4, y));
    }
  }
  drain();
  bar();
  if (tid == 0) st4(ws, OFF_FY + j * 128, sp.enc(0));
  if (w == 0) {
    sp.wait_all(OFF_FY, 28, lane);
    f32x4 y = {0, 0, 0, 0};
#pragma unroll
    for (int k = 0; k < 14; ++k) y += __builtin_bit_cast(f32x4, ld16(ws, OFF_YP + (u32)(j * 28 + 14 * (lane >> 5) + k) * 512 + (lane & 31) * 16));
    const int tk = (lane & 31) >> 2;
    u32 row = sel[0];
#pragma unroll
    for (int r = 1; r < 8; ++r) row = tk == r ? sel[r] : row;
    const float rs = bfr(sigmoidf_(bflo(*(LAS u32*)(lds + F_RG + row * 4))));
    const u32x2 xv = *(const u32x2*)(P.x + (size_t)row * DM + 16 * j + 4 * (lane & 3));
    float b[4];
#pragma unroll
    for (int r = 0; r < 4; ++r) {
      const float t = bfr(bfr(swadd32(y[r], y[r])) * rs);
      const float xe = (r & 1) ? bfhi(xv[r >> 1]) : bflo(xv[r >> 1]);
      b[r] = xe + t;
    }
    if (lane < 32) *(LAS u32x2*)(lds + F_BS + lane * 8) = (u32x2){pk2(b[0], b[1]), pk2(b[2], b[3])};
  }
  const int erow = n + 16 * (w >> 2), ecol = 16 * j + 4 * g + (w & 3);
  int qaseen = -1;
  for (int s = 0; s < NSEG; ++s) {
    const int row = 32 * s + erow;
    const u16 xe = P.x[(size_t)row * DM + ecol];
    if (s < NSEG - 2) {
      u32 nn = 0;
      while (__builtin_amdgcn_ballot_w64(lane < 8 && (lane & 1) == (s & 1) && qaseen < s) != 0) {
        if (nn) __builtin_amdgcn_s_sleep(1);
        if (sp.fail(nn)) break;
        asm volatile("" ::: "memory");
        qaseen = sp.dec(ld4(ws, OFF_FQA + (8 * w + (lane & 7)) * 128));
      }
    }
    u32x4 v[4];
    for (u32 nn = 0;;) {
      asm volatile("" ::: "memory");
#pragma unroll
      for (int i = 0; i < 4; ++i) v[i] = ld16s(ws, lane * 16 + i * 1024, OFF_XP + (u32)((s * 32 + j) * 32 + 4 * w) * 1024);
      if (s < NSEG - 2 || !any_sent(umax4(umax4(v[0], v[1]), umax4(v[2], v[3])))) break;
      if (sp.fail(nn)) break;
    }
    ldsp red = lds + (s & 1) * 16384;
#pragma unroll
    for (int k = 0; k < 4; ++k) {
      *(LAS float*)(red + (2 * k) * 2048 + tid * 4) = (bflo(v[0][k]) + bflo(v[1][k])) + (bflo(v[2][k]) + bflo(v[3][k]));
      *(LAS float*)(red + (2 * k + 1) * 2048 + tid * 4) = (bfhi(v[0][k]) + bfhi(v[1][k])) + (bfhi(v[2][k]) + bfhi(v[3][k]));
    }
    bar();
    float a = 0.f;
#pragma unroll
    for (int k = 0; k < 8; ++k) a += *(LAS float*)(red + w * 2048 + k * 256 + lane * 4);
    const float gt = bfhi(*(LAS u32*)(lds + F_RG + row * 4));
    int hit = -1;
#pragma unroll
    for (int r = 0; r < 8; ++r) hit = (u32)row == sel[r] ? r : hit;
    u32 be = xe;
    if (hit >= 0) be = *(LAS u16*)(lds + F_BS + (hit * 16 + 4 * g + (w & 3)) * 2);
    const float res = bfr(gt * bfr(a)) + bfr(bfr(1.0f - gt) * bflo(be));
    P.out[(size_t)row * DM + ecol] = (u16)pk2(res, res);
  }
}

__global__ void __launch_bounds__(512, 2) rmt_mod_kernel(Params P) {
  extern __shared__ __attribute__((aligned(16))) unsigned char smem[];
  ldsp lds = (ldsp)smem;
  const int tid = threadIdx.x;
  const rsrc_t ws = __builtin_amdgcn_make_buffer_rsrc(P.ws, 0, WS_USED, 0x00020000);
  const int guess = (int)(((u32)__builtin_amdgcn_s_getreg(20 | (3 << 11)) & 7u) * 32 + (blockIdx.x >> 3));
  const bool spec = (guess & 31) < (guess < 128 ? 16 : 8);
  const int wv_ = __builtin_amdgcn_readfirstlane(tid >> 6);
  u32 old = SENT;
  if (tid == 0) {
    GAS u32* cw = (GAS u32*)(P.ws + OFF_CLAIM + guess * 128);
    asm volatile("global_atomic_swap %0, %1, %2, off sc0" : "=&v"(old) : "v"(cw), "v"(0u) : "memory");
  }
  if (spec) {
    if (guess < 128) {
      auto is = [&](auto C, ldsp slot) { chain_stage<C.value>(P, 8 * (guess >> 5) + ((guess & 31) >> 1), guess & 1, slot, wv_, tid & 63); };
      ring_open(lds, is);
    } else {
      auto is = [&](auto C, ldsp slot) { kv_stage<C.value>(P, 8 * ((guess >> 5) - 4) + (guess & 7), slot, wv_, tid & 63); };
      ring_open(lds, is);
    }
  }
  if (wv_ == 0) {
    if (spec) asm volatile("s_waitcnt vmcnt(16)" : "+v"(old) :: "memory");
    else asm volatile("s_waitcnt vmcnt(0)" : "+v"(old) :: "memory");
    if (tid == 0) {
      u32 sl = (u32)guess;
      for (u32 k = 0; k < 256 && old != SENT; ++k) {
        sl = (sl + 1) & 255u;
        old = __hip_atomic_exchange((GAS u32*)(P.ws + OFF_CLAIM + sl * 128), 0u, __ATOMIC_RELAXED, __HIP_MEMORY_SCOPE_AGENT);
      }
      *(LAS u32*)(lds + 131072) = sl;
    }
  }
  bar();
  const int slot = __builtin_amdgcn_readfirstlane(*(LAS u32*)(lds + 131072));
  const bool opened = spec && slot == guess;
  if (spec && !opened) drain();
  bar();
  const int x = slot >> 5, t = slot & 31;
  const u32 tin = now32();
  if (x < 4) {
    if (t < 16) role_chain(P, ws, opened, lds, 8 * x + (t >> 1), t & 1, tid);
    else {
      if (x == 0 && t == 16 && tid >= 256) wait_time(now32() + 120);
      bulk_prefetch(P, x * 16 + t - 16, lds, tid);
      if (t < 24) role_reduce(P, ws, 8 * x + (t - 16), tid);
    }
  } else {
    const int i = 8 * (x - 4) + (t & 7);
    if (t >= 8) bulk_prefetch(P, 64 + (x - 4) * 24 + t - 8, lds + LDS_BYTES - 2048, tid);
    if (t < 8) role_kv(P, ws, opened, lds, i, tid);
    else if (t < 16) role_final(P, ws, tin, lds, i, tid);
    else role_qa(P, ws, tin, lds, i, (t >> 3) & 1, tid);
  }
}

extern "C" void kernel_launch(void* const* d_in, const int* in_sizes, int n_in, void* d_out, int out_size, void* d_ws,
                              size_t ws_size, hipStream_t stream) {
  Params P;
  P.x = (const u16*)d_in[0]; P.mem0 = (const u16*)d_in[1];
  P.wq = (const u16*)d_in[2]; P.wk = (const u16*)d_in[3]; P.wv = (const u16*)d_in[4]; P.wo = (const u16*)d_in[5];
  P.wr = (const u16*)d_in[8]; P.wg = (const u16*)d_in[9]; P.wu = (const u16*)d_in[10]; P.wd = (const u16*)d_in[11];
  P.gw = (const u16*)d_in[12];
  P.out = (u16*)d_out;
  P.ws = (unsigned char*)(((uintptr_t)d_ws + ((2u << 20) - 1)) & ~(uintptr_t)((2u << 20) - 1));
  (void)hipFuncSetAttribute((const void*)rmt_mod_kernel, hipFuncAttributeMaxDynamicSharedMemorySize, LDS_BYTES);
  (void)hipMemsetAsync(P.ws, 0xFF, CTRL_BYTES, stream);
  hipLaunchKernelGGL(rmt_mod_kernel, dim3(256), dim3(512), LDS_BYTES, stream, P);
}
```
